```python
import torch

def _softmax(x, axis=-1):
    return torch.softmax(x, dim=axis)
def _gelu(x):
    return torch.nn.functional.gelu(x, approximate="tanh")
def _silu(x):
    return torch.nn.functional.silu(x)
def _sigmoid(x):
    return torch.sigmoid(x)
def _layernorm(x, axis=-1, eps=1e-5):
    m = x.mean(dim=axis, keepdim=True)
    v = x.var(dim=axis, keepdim=True, unbiased=False)
    return (x - m) / torch.sqrt(v + eps)
def _scatter_add(tgt, idx, src, axis=0):
    return tgt.clone().index_add_(axis, idx, src)
def _segment_sum(src, seg, n):
    out = torch.zeros((n,) + src.shape[1:], dtype=src.dtype)
    return out.index_add_(0, seg, src)
def _pad(x, pw):
    flat = tuple(v for pair in reversed(pw) for v in pair)
    return torch.nn.functional.pad(x, flat)

kernel_name = 'sequential_preLN__mha_block_cached__swiglu_ffn__decode'

def setup_inputs():

    _w = (lambda a: (lambda t: t.to(torch.bfloat16) if t.is_floating_point() else t)(torch.as_tensor(a)))
    def _A_setup():
        B, Sq, D = 1, 1, 512
        H, Sk, Dh = 32, 16384, 128
        return {
            "x": _w((lambda *s: torch.randn(*s, dtype=torch.bfloat16) * 0.05)(B, Sq, D)),
            "k_cache": _w((lambda *s: torch.randn(*s, dtype=torch.bfloat16) * 3.2)(B, H, Sk, Dh)),
            "v_cache": _w((lambda *s: torch.randn(*s, dtype=torch.bfloat16) * 0.4)(B, H, Sk, Dh)),
            "wq": _w((lambda *s: torch.randn(*s, dtype=torch.bfloat16) * 3.2)(D, H * Dh) * (D ** -0.5)),
            "wo": _w((lambda *s: torch.randn(*s, dtype=torch.bfloat16) * 0.4)(H * Dh, D) * ((H * Dh) ** -0.5)),
        }
    _A_in = _A_setup()

    def _B_setup():
        B, S, D, F = 1, 1, 512, 1792
        return {
            "x": _w((lambda *s: torch.randn(*s, dtype=torch.bfloat16) * 0.05)(B, S, D)),
            "w_gate": _w((lambda *s: torch.randn(*s, dtype=torch.bfloat16) * 0.05)(D, F)),
            "w_up": _w((lambda *s: torch.randn(*s, dtype=torch.bfloat16) * 0.05)(D, F)),
            "w_down": _w((lambda *s: torch.randn(*s, dtype=torch.bfloat16) * 0.05)(F, D)),
        }
    _B_in = _B_setup()

    return {
        "A_x": _A_in["x"],
        "A_k_cache": _A_in["k_cache"],
        "A_v_cache": _A_in["v_cache"],
        "A_wq": _A_in["wq"],
        "A_wo": _A_in["wo"],
        "B_x": _B_in["x"],
        "B_w_gate": _B_in["w_gate"],
        "B_w_up": _B_in["w_up"],
        "B_w_down": _B_in["w_down"],
    }

def reference(A_x, A_k_cache, A_v_cache, A_wq, A_wo, B_x, B_w_gate, B_w_up, B_w_down):
    def _A_ref(x, k_cache, v_cache, wq, wo):
        B, Sq, D = x.shape
        H, Dh = 32, 128
        q = (lambda x, *d: x.permute(*d))((x @ wq).reshape(B, Sq, H, Dh), 0, 2, 1, 3)
        scores = torch.einsum("bhqd,bhkd->bhqk", q, k_cache) * (Dh ** -0.5)
        p = _softmax(scores, axis=-1)
        o = torch.einsum("bhqk,bhkd->bhqd", p, v_cache)
        return (lambda x, *d: x.permute(*d))(o, 0, 2, 1, 3).reshape(B, Sq, H * Dh) @ wo

    def _B_ref(x, w_gate, w_up, w_down):
        g = _silu(x @ w_gate)
        u = x @ w_up
        return (g * u) @ w_down

    x = A_x
    h = x + _A_ref(_layernorm(x, axis=-1), A_k_cache, A_v_cache, A_wq, A_wo)
    h2 = h + _B_ref(_layernorm(h, axis=-1), B_w_gate, B_w_up, B_w_down)
    return h2
```

```cpp
#include <hip/hip_runtime.h>
#include <hip/hip_bf16.h>
#include <stdint.h>

typedef unsigned short u16;
typedef unsigned u32x4 __attribute__((ext_vector_type(4)));
typedef __bf16 bf16x2 __attribute__((ext_vector_type(2)));

constexpr int D = 512, H = 32, SK = 16384, DH = 128, F = 1792;
constexpr int NSPLIT = 8;

__device__ __forceinline__ float bf2f(u16 v) { return __uint_as_float(((unsigned)v) << 16); }
__device__ __forceinline__ float bflo(unsigned v) { return __uint_as_float(v << 16); }
__device__ __forceinline__ float bfhi(unsigned v) { return __uint_as_float(v & 0xffff0000u); }
__device__ __forceinline__ u16 f2bf(float f) { __bf16 b = (__bf16)f; return __builtin_bit_cast(u16, b); }
__device__ __forceinline__ float rbf(float f) { return bf2f(f2bf(f)); }
__device__ __forceinline__ float dot2(unsigned a, unsigned b, float acc) {
  return __builtin_amdgcn_fdot2_f32_bf16(__builtin_bit_cast(bf16x2, a), __builtin_bit_cast(bf16x2, b), acc, false);
}
__device__ __forceinline__ float wave_sum(float v) {
#pragma unroll
  for (int m = 32; m >= 1; m >>= 1) v += __shfl_xor(v, m);
  return v;
}
__device__ __forceinline__ float wave_max(float v) {
#pragma unroll
  for (int m = 32; m >= 1; m >>= 1) v = fmaxf(v, __shfl_xor(v, m));
  return v;
}

__device__ void layernorm512(const float* xs, float* ln, float* red, int tid) {
  float a = xs[tid], b = xs[tid + 256];
  float s = wave_sum(a + b);
  if ((tid & 63) == 0) red[tid >> 6] = s;
  __syncthreads();
  float mean = (red[0] + red[1] + red[2] + red[3]) * (1.f / 512.f);
  __syncthreads();
  float da = a - mean, db = b - mean;
  float v = wave_sum(da * da + db * db);
  if ((tid & 63) == 0) red[tid >> 6] = v;
  __syncthreads();
  float var = (red[0] + red[1] + red[2] + red[3]) * (1.f / 512.f);
  float mb = rbf(mean), vb = rbf(var);
  float den = rbf(sqrtf(rbf(vb + 1e-5f)));
  ln[tid] = rbf(rbf(a - mb) / den);
  ln[tid + 256] = rbf(rbf(b - mb) / den);
  __syncthreads();
}

__global__ void __launch_bounds__(256) k_q(const u16* x, const u16* wq, u16* q) {
  __shared__ float xs[512], ln[512], red[4], part[4][64];
  int tid = threadIdx.x;
  xs[tid] = bf2f(x[tid]); xs[tid + 256] = bf2f(x[tid + 256]);
  __syncthreads();
  layernorm512(xs, ln, red, tid);
  int col = blockIdx.x * 64 + (tid & 63), w = tid >> 6;
  float acc = 0.f;
  for (int k = w * 128; k < w * 128 + 128; ++k) acc += ln[k] * bf2f(wq[(size_t)k * 4096 + col]);
  part[w][tid & 63] = acc;
  __syncthreads();
  if (tid < 64) q[col] = f2bf(part[0][tid] + part[1][tid] + part[2][tid] + part[3][tid]);
}

__global__ void __launch_bounds__(256) k_attn(const u16* q, const u16* K, const u16* V, float* pm, float* pl, float* po) {
  __shared__ float sm[4], sl[4], so[4][128];
  int h = blockIdx.x / NSPLIT, s = blockIdx.x % NSPLIT;
  int tid = threadIdx.x, lane = tid & 63, w = tid >> 6, r = lane >> 4, c = lane & 15;
  constexpr int KPS = SK / NSPLIT;
  u32x4 qv = *(const u32x4*)(q + h * 128 + c * 8);
  const u16* kb = K + ((size_t)h * SK + s * KPS) * DH;
  const u16* vb = V + ((size_t)h * SK + s * KPS) * DH;
  float m = -INFINITY, l = 0.f, acc[8];
  for (int i = 0; i < 8; ++i) acc[i] = 0.f;
  for (int t = w * 16; t < KPS; t += 64) {
    u32x4 kk[4], vv[4];
#pragma unroll
    for (int j = 0; j < 4; ++j) {
      kk[j] = __builtin_nontemporal_load((const u32x4*)(kb + (size_t)(t + j * 4 + r) * DH + c * 8));
      vv[j] = __builtin_nontemporal_load((const u32x4*)(vb + (size_t)(t + j * 4 + r) * DH + c * 8));
    }
    float sc[4];
#pragma unroll
    for (int j = 0; j < 4; ++j) {
      float d = 0.f;
      d = dot2(kk[j][0], qv[0], d); d = dot2(kk[j][1], qv[1], d); d = dot2(kk[j][2], qv[2], d); d = dot2(kk[j][3], qv[3], d);
      d += __shfl_xor(d, 1); d += __shfl_xor(d, 2); d += __shfl_xor(d, 4); d += __shfl_xor(d, 8);
      sc[j] = d * 0.08838834764831845f;
    }
    float mx = fmaxf(fmaxf(sc[0], sc[1]), fmaxf(sc[2], sc[3]));
    float mn = fmaxf(m, mx);
    float al = __expf(m - mn);
    l *= al;
#pragma unroll
    for (int i = 0; i < 8; ++i) acc[i] *= al;
#pragma unroll
    for (int j = 0; j < 4; ++j) {
      float p = __expf(sc[j] - mn);
      l += p;
#pragma unroll
      for (int i = 0; i < 4; ++i) { acc[2 * i] += p * bflo(vv[j][i]); acc[2 * i + 1] += p * bfhi(vv[j][i]); }
    }
    m = mn;
  }
  float M = fmaxf(m, __shfl_xor(m, 16)); M = fmaxf(M, __shfl_xor(M, 32));
  float f = __expf(m - M);
  l *= f; l += __shfl_xor(l, 16); l += __shfl_xor(l, 32);
#pragma unroll
  for (int i = 0; i < 8; ++i) { acc[i] *= f; acc[i] += __shfl_xor(acc[i], 16); acc[i] += __shfl_xor(acc[i], 32); }
  if (lane == 0) { sm[w] = M; sl[w] = l; }
  if (r == 0) for (int i = 0; i < 8; ++i) so[w][c * 8 + i] = acc[i];
  __syncthreads();
  if (tid < 128) {
    float MM = fmaxf(fmaxf(sm[0], sm[1]), fmaxf(sm[2], sm[3]));
    float L = 0.f, o = 0.f;
    for (int ww = 0; ww < 4; ++ww) { float g = __expf(sm[ww] - MM); L += g * sl[ww]; o += g * so[ww][tid]; }
    po[blockIdx.x * 128 + tid] = o;
    if (tid == 0) { pm[blockIdx.x] = MM; pl[blockIdx.x] = L; }
  }
}

__global__ void __launch_bounds__(256) k_wo(const float* pm, const float* pl, const float* po, const u16* wo, float* y) {
  __shared__ float o[128];
  int h = blockIdx.x, tid = threadIdx.x;
  if (tid < 128) {
    float M = -INFINITY;
    for (int s = 0; s < NSPLIT; ++s) M = fmaxf(M, pm[h * NSPLIT + s]);
    float L = 0.f, a = 0.f;
    for (int s = 0; s < NSPLIT; ++s) { float g = __expf(pm[h * NSPLIT + s] - M); L += g * pl[h * NSPLIT + s]; a += g * po[(h * NSPLIT + s) * 128 + tid]; }
    o[tid] = rbf(a / L);
  }
  __syncthreads();
  float a0 = 0.f, a1 = 0.f;
  for (int k = 0; k < 128; ++k) {
    unsigned wv = *(const unsigned*)(wo + (size_t)(h * 128 + k) * 512 + tid * 2);
    a0 += o[k] * bflo(wv); a1 += o[k] * bfhi(wv);
  }
  y[h * 512 + tid * 2] = a0; y[h * 512 + tid * 2 + 1] = a1;
}

__global__ void __launch_bounds__(256) k_gu(const u16* x, const float* y, const u16* wg, const u16* wu, float* hbuf, u16* act) {
  __shared__ float xs[512], ln[512], red[4], pg[4][64], pu[4][64];
  int tid = threadIdx.x;
  for (int j = tid; j < 512; j += 256) {
    float a = 0.f;
    for (int h = 0; h < H; ++h) a += y[h * 512 + j];
    xs[j] = rbf(bf2f(x[j]) + rbf(a));
    if (blockIdx.x == 0) hbuf[j] = xs[j];
  }
  __syncthreads();
  layernorm512(xs, ln, red, tid);
  int col = blockIdx.x * 64 + (tid & 63), w = tid >> 6;
  float g = 0.f, u = 0.f;
  for (int k = w * 128; k < w * 128 + 128; ++k) { g += ln[k] * bf2f(wg[(size_t)k * F + col]); u += ln[k] * bf2f(wu[(size_t)k * F + col]); }
  pg[w][tid & 63] = g; pu[w][tid & 63] = u;
  __syncthreads();
  if (tid < 64) {
    g = rbf(pg[0][tid] + pg[1][tid] + pg[2][tid] + pg[3][tid]);
    u = rbf(pu[0][tid] + pu[1][tid] + pu[2][tid] + pu[3][tid]);
    float sg = rbf(g / (1.f + __expf(-g)));
    act[col] = f2bf(sg * u);
  }
}

__global__ void __launch_bounds__(256) k_down(const float* hbuf, const u16* act, const u16* wd, u16* out) {
  __shared__ float part[4][64];
  int tid = threadIdx.x, col = blockIdx.x * 64 + (tid & 63), w = tid >> 6;
  float a = 0.f;
  for (int k = w * 448; k < w * 448 + 448; ++k) a += bf2f(act[k]) * bf2f(wd[(size_t)k * 512 + col]);
  part[w][tid & 63] = a;
  __syncthreads();
  if (tid < 64) out[col] = f2bf(hbuf[col] + rbf(part[0][tid] + part[1][tid] + part[2][tid] + part[3][tid]));
}

extern "C" void kernel_launch(void* const* d_in, const int* in_sizes, int n_in, void* d_out, int out_size, void* d_ws, size_t ws_size, hipStream_t stream) {
  const u16* x = (const u16*)d_in[0];
  const u16* K = (const u16*)d_in[1];
  const u16* V = (const u16*)d_in[2];
  const u16* wq = (const u16*)d_in[3];
  const u16* wo = (const u16*)d_in[4];
  const u16* wg = (const u16*)d_in[6];
  const u16* wu = (const u16*)d_in[7];
  const u16* wd = (const u16*)d_in[8];
  char* ws = (char*)d_ws;
  u16* q = (u16*)ws;
  float* pm = (float*)(ws + 8192);
  float* pl = (float*)(ws + 12288);
  float* po = (float*)(ws + 16384);
  float* y = (float*)(ws + 16384 + 131072);
  float* hbuf = (float*)(ws + 16384 + 131072 + 65536);
  u16* act = (u16*)(ws + 16384 + 131072 + 65536 + 4096);
  k_q<<<64, 256, 0, stream>>>(x, wq, q);
  k_attn<<<H * NSPLIT, 256, 0, stream>>>(q, K, V, pm, pl, po);
  k_wo<<<32, 256, 0, stream>>>(pm, pl, po, wo, y);
  k_gu<<<28, 256, 0, stream>>>(x, y, wg, wu, hbuf, act);
  k_down<<<8, 256, 0, stream>>>(hbuf, act, wd, (u16*)d_out);
}
```

```cpp
#define PRE 3
#include <hip/hip_runtime.h>
#include <stdint.h>
#include <utility>

typedef unsigned short u16;
typedef unsigned long long u64;
typedef unsigned u32x2 __attribute__((ext_vector_type(2)));
typedef unsigned u32x4 __attribute__((ext_vector_type(4)));
typedef unsigned u32x8 __attribute__((ext_vector_type(8)));
typedef unsigned u32x16 __attribute__((ext_vector_type(16)));
typedef unsigned u32x32 __attribute__((ext_vector_type(32)));
typedef float f32x2 __attribute__((ext_vector_type(2)));
typedef float f32x4 __attribute__((ext_vector_type(4)));
typedef __bf16 bf16x2 __attribute__((ext_vector_type(2)));
typedef __bf16 bf16x8 __attribute__((ext_vector_type(8)));
typedef __bf16 bf16x16 __attribute__((ext_vector_type(16)));
#define LAS __attribute__((address_space(3)))
#define DI __device__ __forceinline__
#define SB() __builtin_amdgcn_sched_barrier(0)
#define FOR(i, n) _Pragma("unroll") for (int i = 0; i < (n); ++i)

#define DMAPOL ""
#define KPOL "nt"
#define VPOL "nt"
#define QPOL "nt"
#define PW 1
#define PWIN 6
#define QD0 18
#define QD1 10
#define WB4 22
#define GI 40
#define HMIN 450
#define TSH 400
#define KW 0
#define TK 14
#define TV 14
#define B0 560
#define DB 370
#define NB 8
#define CTL2 0
#define HC 400
#define TI 800
#define LPF 0.3f
#define RHO0 0.94f
#define PWARM 0
#define YLATE 1
#define YD0 0
#define DPRE 1
#define AD0 0
#define WD0 0
#define WRST 0
#define RSM 1
#define RW0 400
#define CMR 1
#define U0 0.8f
#define LOFF 0.f
#define RD1 100
#define RD2 200
#define WLO 14
#define WHI 34
#define PEND 224
#define SLP 4
#define RDY0 350
#define MLD 400
#define GWG 3
#define TG 38
#define UG 1
#define UD 2
#define PB1 0
#define PB2 0
#define PB3 0
#define PB4 2
#define GD0 3
#define GWD 1
#define GWIN 1
#define DROT 0
#define WROT 0
#define GROT 0
#define QI 4
#define DPF 0
#define DLAG 6
#define DSW 0
#define DMSK 63
#define DSPL 0
#define DSL 0
#define UD3 UD
#define TD 48
#define C3 1
#define CWIN 3
#define UC 1
#define FFW 0
#define WOV 0
#define EB 120
#define PBS 218
#define NPO 0
#define NPY 0
#define NPA 0
#define LG 1
#define ROA 0
#define ROS 2
#define ROB 4
#define NPS 0
#define PRD 100
#define PTG 232
#define WMX 176
#define WV7 15
#define KB 8
#define EPF 0
#define GL 0
#define TSG 60
#define TSD 60
#define GDL 0
#define DDL 0
#define CPF0 0
#define CPF1 0
#define SFIRST 0

constexpr int DM = 512, NH = 32, SK = 16384, DH = 128, FF = 1792;
constexpr unsigned SENT = 0xFFFFFFFFu;
constexpr int SPIN_MAX = 1 << 18;

constexpr size_t WS_EPOCH = 0, WS_READY = 4096, WS_SIDE = 7168, WS_ML = 49152, WS_PO = 65536, WS_Y = 16384, WS_ACT = 147456, WS_H = WS_ACT + 4096, WS_ROW = 8192, WS_PRB = 196608, WS_Q = 262144, WS_CF = 131072, WS_C3 = 524288;
constexpr size_t WS_STAMP = 1 << 20;
constexpr int L_W = 0, L_G = 16384, L_QP = L_G + 65536, L_OP = L_QP + 2048, L_H = L_OP + 2048, L_DP = L_H + 1024,
              L_QB = L_DP + 1024, L_ML = L_QB + 256, L_CNT = L_ML + 64, L_FIN = L_CNT + 16, L_MODE = L_FIN + 16, L_EP = L_MODE + 16, L_PROG = L_EP + 16, L_END = L_PROG + 1024;
static_assert(L_END > 81920, "one workgroup per CU");

template <int N> using IC = std::integral_constant<int, N>;
template <int... I, class F> DI void sfor_impl(std::integer_sequence<int, I...>, F&& f) { (f(IC<I>{}), ...); }
template <int N, class F> DI void sfor(F&& f) { sfor_impl(std::make_integer_sequence<int, N>{}, f); }

DI float bflo(unsigned v) { return __uint_as_float(v << 16); }
DI float bfhi(unsigned v) { return __uint_as_float(v & 0xffff0000u); }
DI unsigned pack2(float a, float b) { bf16x2 r = {(__bf16)a, (__bf16)b}; return __builtin_bit_cast(unsigned, r); }
DI float rbf(float a) { return bflo(pack2(a, a)); }
DI float dot2(unsigned a, unsigned b, float c) {
  return __builtin_amdgcn_fdot2_f32_bf16(__builtin_bit_cast(bf16x2, a), __builtin_bit_cast(bf16x2, b), c, false);
}
DI f32x4 mfma(u32x4 a, u32x4 b, f32x4 c) {
  return __builtin_amdgcn_mfma_f32_16x16x32_bf16(__builtin_bit_cast(bf16x8, a), __builtin_bit_cast(bf16x8, b), c, 0, 0, 0);
}
DI f32x4 smf(u32x4 a, u32x8 b, f32x4 c, unsigned idx) {
  return __builtin_amdgcn_smfmac_f32_16x16x64_bf16(__builtin_bit_cast(bf16x8, a), __builtin_bit_cast(bf16x16, b), c, idx, 0, 0);
}
template <int CTRL> DI unsigned dppu(unsigned v) { return (unsigned)__builtin_amdgcn_update_dpp(0, (int)v, CTRL, 0xf, 0xf, true); }
template <int CTRL> DI float dppf(float v) { return __uint_as_float(dppu<CTRL>(__float_as_uint(v))); }
constexpr int ROR = 0x120, NBC = 0x150, QX1 = 0xB1, QX2 = 0x4E, HMIR = 0x141;
DI float row_sum(float v) { v += dppf<ROR + 8>(v); v += dppf<ROR + 4>(v); v += dppf<ROR + 2>(v); v += dppf<ROR + 1>(v); return v; }
DI float row_max(float v) { v = fmaxf(v, dppf<ROR + 8>(v)); v = fmaxf(v, dppf<ROR + 4>(v)); v = fmaxf(v, dppf<ROR + 2>(v)); v = fmaxf(v, dppf<ROR + 1>(v)); return v; }
DI f32x2 swap16(float v) { auto r = __builtin_amdgcn_permlane16_swap(__float_as_uint(v), __float_as_uint(v), false, false); return f32x2{__uint_as_float(r[0]), __uint_as_float(r[1])}; }
DI f32x2 swap32(float v) { auto r = __builtin_amdgcn_permlane32_swap(__float_as_uint(v), __float_as_uint(v), false, false); return f32x2{__uint_as_float(r[0]), __uint_as_float(r[1])}; }
DI float wave_sum(float v) { v = row_sum(v); f32x2 a = swap16(v); v = a[0] + a[1]; a = swap32(v); return a[0] + a[1]; }
DI float wave_max(float v) { v = row_max(v); f32x2 a = swap16(v); v = fmaxf(a[0], a[1]); a = swap32(v); return fmaxf(a[0], a[1]); }
DI unsigned canon(unsigned v) { return min(v, SENT - 1); }
DI unsigned umax3(unsigned a, unsigned b, unsigned c) { return max(max(a, b), c); }
DI const char* uni(const char* p) {
  u64 v = (u64)p;
  unsigned lo = __builtin_amdgcn_readfirstlane((unsigned)v), hi = __builtin_amdgcn_readfirstlane((unsigned)(v >> 32));
  return (const char*)(((u64)hi << 32) | lo);
}

template <int OFF> DI void ld_nt(u32x4& d, unsigned voff, const char* sb) {
  asm volatile("global_load_dwordx4 %0, %1, %2 offset:%3 " KPOL : "+v"(d) : "v"(voff), "s"(sb), "i"(OFF) : "memory");
}
template <int OFF> DI void ld_nt0(u32x4& d, unsigned voff, const char* sb) {
  asm volatile("global_load_dwordx4 %0, %1, %2 offset:%3 " KPOL : "=v"(d) : "v"(voff), "s"(sb), "i"(OFF) : "memory");
}
template <int OFF> DI void ld_q0(u32x4& d, unsigned voff, const char* sb) {
  asm volatile("global_load_dwordx4 %0, %1, %2 offset:%3 " QPOL : "=v"(d) : "v"(voff), "s"(sb), "i"(OFF) : "memory");
}
template <int N, int OFF> DI void qstep(f32x4& acc, u32x4 a, u32x4& d, unsigned voff, const char* sb) {
  asm volatile("s_waitcnt vmcnt(%5)\n\ts_nop 0\n\tv_mfma_f32_16x16x32_bf16 %0, %2, %1, %0\n\tglobal_load_dwordx4 %1, %3, %4 offset:%6 " QPOL
               : "+v"(acc), "+v"(d) : "v"(a), "v"(voff), "s"(sb), "i"(N), "i"(OFF) : "memory");
}
template <int OFF> DI void ld_sc1(u32x4& d, unsigned voff, const char* sb) {
  asm volatile("global_load_dwordx4 %0, %1, %2 offset:%3 sc1" : "=v"(d) : "v"(voff), "s"(sb), "i"(OFF) : "memory");
}
template <int N, class T> DI void waitv(T& d) { asm volatile("s_waitcnt vmcnt(%1)" : "+v"(d) : "i"(N) : "memory"); }
template <int N> DI void waitv() { asm volatile("s_waitcnt vmcnt(%0)" ::"i"(N) : "memory"); }
template <class T> DI void pin(T& d) { asm volatile("" : "+v"(d)); }
#define VW1(md, n, k) "s_waitcnt vmcnt(" #n ")\n\ts_cmp_gt_u32 " md ", " #k "\n\ts_cbranch_scc1 LVW%=\n\t"
#define VW0(md) VW1x(md, WV7, 6)
#define VW1x(md, n, k) VW1(md, n, k)
#define VWAIT(md) VW0(md) VW1(md, 9, 5) VW1(md, 8, 4) VW1(md, 7, 3) VW1(md, 6, 2) VW1(md, 5, 1) VW1(md, 4, 0) "s_waitcnt vmcnt(3)\nLVW%=:"
template <class T> DI void waitm(T& d, int md) { asm volatile(VWAIT("%1") : "+v"(d) : "s"(md) : "memory", "scc"); }
template <int OFF> DI void vstepm(f32x4& acc, u32x4 a, u32x4& d, unsigned voff, const char* sb, int md) {
  asm volatile(VWAIT("%5") "\n\tv_mfma_f32_16x16x32_bf16 %0, %2, %1, %0\n\tglobal_load_dwordx4 %1, %3, %4 offset:%6 " VPOL
               : "+v"(acc), "+v"(d) : "v"(a), "v"(voff), "s"(sb), "s"(md), "i"(OFF) : "memory", "scc");
}
template <int N, int OFF> DI void vstep(f32x4& acc, u32x4 a, u32x4& d, unsigned voff, const char* sb) {
  asm volatile("s_waitcnt vmcnt(%5)\n\ts_nop 0\n\tv_mfma_f32_16x16x32_bf16 %0, %2, %1, %0\n\tglobal_load_dwordx4 %1, %3, %4 offset:%6 " VPOL
               : "+v"(acc), "+v"(d) : "v"(a), "v"(voff), "s"(sb), "i"(N), "i"(OFF) : "memory");
}
template <int N> DI void vlast(f32x4& acc, u32x4 a, const u32x4& d) {
  asm volatile("s_waitcnt vmcnt(%3)\n\ts_nop 0\n\tv_mfma_f32_16x16x32_bf16 %0, %2, %1, %0" : "+v"(acc) : "v"(d), "v"(a), "i"(N) : "memory");
}
DI void dma(const char* src, unsigned dst) {
  unsigned keep;
  asm volatile("s_mov_b32 %0, m0\n\ts_mov_b32 m0, %2\n\ts_nop 0\n\tglobal_load_lds_dwordx4 %1, off " DMAPOL "\n\ts_mov_b32 m0, %0"
               : "=&s"(keep) : "v"(src), "s"(dst) : "memory");
}
DI void dma_sc1(const char* src, unsigned dst) {
  unsigned keep;
  asm volatile("s_mov_b32 %0, m0\n\ts_mov_b32 m0, %2\n\ts_nop 0\n\tglobal_load_lds_dwordx4 %1, off sc1\n\ts_mov_b32 m0, %0"
               : "=&s"(keep) : "v"(src), "s"(dst) : "memory");
}
DI void st4_sc1(unsigned voff, const char* sb, u32x4 v) { asm volatile("global_store_dwordx4 %0, %1, %2 sc1\n\ts_nop 1" ::"v"(voff), "v"(v), "s"(sb) : "memory"); }
DI void st2_sc1(unsigned voff, const char* sb, u32x2 v) { asm volatile("global_store_dwordx2 %0, %1, %2 sc1" ::"v"(voff), "v"(v), "s"(sb) : "memory"); }
DI void st1_sc1(unsigned voff, const char* sb, unsigned v) { asm volatile("global_store_dword %0, %1, %2 sc1" ::"v"(voff), "v"(v), "s"(sb) : "memory"); }
DI void barrier() { asm volatile("s_waitcnt lgkmcnt(0)\n\ts_barrier" ::: "memory"); }
template <int RS> DI u32x8 tr_frag(LAS char* p) {
  typedef short v4i16 __attribute__((ext_vector_type(4)));
  auto rd = [&](int row) { return __builtin_bit_cast(u32x2, __builtin_amdgcn_ds_read_tr16_b64_v4i16((LAS v4i16*)(p + row * RS))); };
  const u32x2 a = rd(0), b = rd(32), c = rd(16), d = rd(48);
  return u32x8{a[0], b[0], a[1], b[1], c[0], d[0], c[1], d[1]};
}
template <int RS, int N> DI u32x32 tr_frags(LAS char* p, int stride) {
  u32x32 f;
  FOR(T, 4) { const u32x8 t = tr_frag<RS>(p + (T < N ? T : 0) * stride); FOR(I, 8) { f[T * 8 + I] = t[I]; } }
  return f;
}
#define KEND0 "LKEND:\n\t"
#define KEND1 ""
DI void code_touch(unsigned& a, unsigned& b, unsigned& c, int lane) {
  asm volatile("s_getpc_b64 vcc\nLPC%=:\n\ts_sub_u32 vcc_lo, vcc_lo, LPC%=-LKBEG\n\ts_subb_u32 vcc_hi, vcc_hi, 0\n\t"
               "v_lshlrev_b32 %0, 7, %3\n\tv_add_u32 %1, 0x2000, %0\n\tv_add_u32 %2, 0x4000, %0\n\t"
               "v_min_u32 %0, LKEND-LKBEG, %0\n\tv_min_u32 %1, LKEND-LKBEG, %1\n\tv_min_u32 %2, LKEND-LKBEG, %2\n\t"
               "global_load_dword %1, %1, vcc\n\tglobal_load_dword %0, %0, vcc\n\tglobal_load_dword %2, %2, vcc"
               : "=&v"(a), "=&v"(b), "=&v"(c) : "v"(lane) : "memory", "scc", "vcc");
}
#define STR_(x) #x
#define STR(x) STR_(x)
#define SMF(d, a, b) "v_smfmac_f32_16x16x64_bf16 v[" d "], v[" a "], v[" b "], %[idx]\n\t"
#define PKA(d, a, b) "v_pk_add_f32 v[" d "], v[" a "], v[" b "]\n\t"
#define SLEEP(n) ".if " STR(n) "\n\ts_sleep " STR(n) "\n\t.endif\n\t"
#define ROWSUM(p, q, a, b) "v_add_f32 " p ", " a ", " b "\n\tv_add_f32 " q ", " a ", " b "\n\ts_nop 1\n\tv_permlane16_swap_b32 " p ", " q "\n\t" \
                           "v_add_f32 " a ", " p ", " q "\n\tv_add_f32 " q ", " p ", " q "\n\ts_nop 1\n\tv_permlane32_swap_b32 " a ", " q "\n\tv_add_f32 " p ", " a ", " q "\n\t"

#define STW 16
#define STAMP(i)
#define STAMPR(i)
#define STAMP_OUT()
#define STAMP_DECL()
#define CNT(i)
#define ACC(i, v)

#define AST(n)
#define ASTEND
#define ASTOUT
#define ASTDECL
#define ASTPUT(i, k)
#define ACNT(i, v)

__global__ void __launch_bounds__(384, 2)
fused_decode_block(const char* x_, const char* kc_, const char* vc_, const char* wq_, const char* wo_, const char* wg_, const char* wu_,
                   const char* wd_, char* out_, char* ws_) {
  STAMP_DECL();
  STAMP(30);
  u32x16 ka; u32x4 kb4;
  asm volatile("LKBEG:\n\ts_load_dwordx16 %0, %2, 0\n\ts_load_dwordx4 %1, %2, 64" : "=&s"(ka), "=&s"(kb4) : "s"(__builtin_amdgcn_kernarg_segment_ptr()) : "memory");
  unsigned cp0 = 0, cp1 = 0, cp2 = 0;
  if (CPF0 && __builtin_amdgcn_readfirstlane(threadIdx.x >> 6) == 5) code_touch(cp0, cp1, cp2, threadIdx.x & 63);
  asm volatile("s_waitcnt lgkmcnt(0)" : "+s"(ka), "+s"(kb4) : : "memory");
  STAMP(31);
  auto P = [](unsigned lo, unsigned hi) { return (const char*)(((u64)hi << 32) | lo); };
  const char *x = P(ka[0], ka[1]), *kc = P(ka[2], ka[3]), *vc = P(ka[4], ka[5]), *wq = P(ka[6], ka[7]), *wo = P(ka[8], ka[9]), *wg = P(ka[10], ka[11]),
             *wu = P(ka[12], ka[13]), *wd = P(ka[14], ka[15]), *out = P(kb4[0], kb4[1]), *ws = P(kb4[2], kb4[3]);
  __shared__ __attribute__((aligned(16))) char smem_[L_END];
  LAS char* sm = (LAS char*)smem_;
  const int tid = threadIdx.x, lane = tid & 63, w = __builtin_amdgcn_readfirstlane(tid >> 6);
  const int b = blockIdx.x, g = b & 7, j = b >> 3;
  const int cb = g * 32 + j, head = cb >> 3, split = cb & 7;
  const int r = lane >> 4, c = lane & 15;
  const bool isG = g < 2 && j < 28, isD = (g >> 1) == 1 && j >= 16;
  const int gidx = g * 28 + j, didx = g * 16 + (j ^ DSW) - 48;
  const int q3 = (3 - ((unsigned)(uintptr_t)wd >> 7)) & 7;
  const bool isC = C3 && isD && (didx >> 2) == q3;
  constexpr float C1 = 0.08838834764831845f * 1.4426950408889634f;

  if (SFIRST ? __builtin_expect(w < 4, 1) : w < 4) {
    const unsigned voff = lane * 16;
    const int qq = cb >> LG;
    const unsigned lo = (qq * 524288u << LG) + (cb & ((1 << LG) - 1)) * 16384u + w * 4096u, rot = qq * ROA + (qq >> ROS) * ROB;
    const char* kb = kc + lo;
    const char* vb = vc + lo;
    auto piece = [&](int i) -> unsigned { return ((rot + i) & 31) * (16384u << LG); };
    u32x4 buf[16], ext[16], xs;
    unsigned xb;
    const int row0 = split * 64 + w * 16;
    asm volatile("global_load_dwordx4 %0, %1, %2" : "=v"(xs) : "v"(voff), "s"(x) : "memory");
    asm volatile("global_load_ushort %0, %1, %2" : "=v"(xb) : "v"((unsigned)((row0 + 4 * (c & 3) + r) * 2)), "s"(x) : "memory");
    unsigned xg;
    const unsigned hoff = w * 256 + (lane & 31) * 8 + (lane >> 5) * 4;
    asm volatile("global_load_dword %0, %1, %2" : "=v"(xg) : "v"(hoff), "s"(x) : "memory");
    const char* qb = wq + head * 256 + row0 * 8192;
    sfor<4>([&](auto J) { ld_q0<0>(ext[12 + J], r * 8192 + c * 16, qb + J * 32768); });
    u32x2 ep;
    if (EPF) asm volatile("global_load_dwordx2 %0, %1, %2 sc1" : "=v"(ep) : "v"(0u), "s"(ws + WS_EPOCH) : "memory");
    sfor<PW>([&](auto N) { ld_nt0<(N & 3) * 1024>(ext[N], voff, kb + piece(N >> 2)); });
    u32x4 msk;
    FOR(I, 4) { msk[I] = (c >> 1) == I ? ((c & 1) ? 0xFFFF0000u : 0x0000FFFFu) : 0u; }
    STAMP(17);
    waitv<4 + EPF + PW>(xs); pin(xb); pin(xg);
    STAMP(18);
    {
      float v[8];
      FOR(I, 4) { v[2 * I] = bflo(xs[I]); v[2 * I + 1] = bfhi(xs[I]); }
      float s = ((v[0] + v[1]) + (v[2] + v[3])) + ((v[4] + v[5]) + (v[6] + v[7]));
      float mean = wave_sum(s) * (1.f / DM);
      float q2 = 0.f;
      FOR(I, 8) { float d = v[I] - mean; q2 = fmaf(d, d, q2); }
      float rstd = __builtin_amdgcn_rsqf(wave_sum(q2) * (1.f / DM) + 1e-5f);
      pin(mean); pin(rstd);
      u32x4 one;
      FOR(I, 4) { one[I] = ((c - 8) >> 1) == I ? ((c & 1) ? 0x3F800000u : 0x00003F80u) : 0u; }
      const unsigned xx = xb | (xb << 16);
      f32x4 aq = {0.f, 0.f, 0.f, 0.f};
      sfor<4>([&](auto J) {
        unsigned xj = dppu<NBC + J>(xx);
        u32x4 a;
        FOR(I, 4) { a[I] = (xj & msk[I]) | one[I]; }
        vlast<3 - J + EPF + PW + (QI > J ? J : QI)>(aq, a, ext[12 + J]);
        if constexpr (J < QI) ld_nt0<((PW + J) & 3) * 1024>(ext[PW + J], voff, kb + piece((PW + J) >> 2));
      });
      asm volatile("s_nop 7\n\ts_nop 3" : "+v"(aq));
      f32x4 val;
      FOR(I, 4) { f32x2 s = swap32(aq[I]); val[I] = fmaf(-mean, s[1], s[0]) * rstd; }
      if (lane < 32) *(LAS f32x4*)(sm + L_QP + w * 512 + c * 32 + r * 16) = val;
      if (EPF) { waitv<PW + QI>(ep); *(LAS u32x2*)(sm + L_EP) = ep; }
    }
    STAMP(14);
    barrier();
    int tick = 0;
    sfor<32 - PW - QI>([&](auto M) {
      constexpr int N = M + PW + QI;
      waitv<PWIN - 1>();
      if constexpr (N < 16) ld_nt0<(N & 3) * 1024>(ext[N], voff, kb + piece(N >> 2));
      else ld_nt0<(N & 3) * 1024>(buf[N - 16], voff, kb + piece(N >> 2));
      if constexpr (N >= 16 && (N & 3) == 0) *(volatile LAS unsigned*)(sm + L_PROG + w * 256 + lane * 4) = ++tick;
    });
    STAMP(21);
    u32x4 qv;
    {
      _Pragma("nounroll") for (int it = 0; it < SPIN_MAX * 4 && *(volatile LAS unsigned*)(sm + L_CNT + 8) == 0; ++it) __builtin_amdgcn_s_sleep(1);
      asm volatile("" ::: "memory");
      qv = *(volatile LAS u32x4*)(sm + L_QB + c * 16);
    }
    STAMP(2);
    float sv[8];
    FOR(I, 8) sv[I] = 0.f;
    int md = WB4 / 4 - 4;
    unsigned mdn = md;
    auto ctl = [&](auto J) {
      if constexpr ((J & 3) == 0) {
        ++tick;
        *(volatile LAS unsigned*)(sm + L_PROG + w * 256 + lane * 4) = tick;
        mdn = *(volatile LAS unsigned*)(sm + L_MODE + w * 4);
      }
      if constexpr ((J & 3) == 2) md = __builtin_amdgcn_readfirstlane(mdn);
    };
    auto score = [&](const u32x4& k) {
      float d = dot2(k[0], qv[0], 0.f);
      d = dot2(k[1], qv[1], d); d = dot2(k[2], qv[2], d);
      return dot2(k[3], qv[3], d);
    };
    auto kgroup = [&](auto FIRST, int gi) {
      const char* nb = gi < 7 ? kb : vb;
      const int i0 = gi < 7 ? gi * 4 + 4 : 0;
      float sc = 0.f, s0 = 0.f;
      sfor<16>([&](auto J) {
        ctl(J);
        waitm(buf[J], md);
        float d = score(buf[J]);
        SB();
        ld_nt<(J & 3) * 1024>(buf[J], voff, nb + piece(i0 + (J >> 2)));
        d = row_sum(d);
        sc = c == J ? d : sc;
        if constexpr (FIRST) { pin(ext[J]); float e = row_sum(score(ext[J])); s0 = c == J ? e : s0; }
      });
      FOR(I, 7) sv[I] = sv[I + 1];
      if constexpr (FIRST) sv[6] = s0;
      sv[7] = sc;
    };
    kgroup(IC<1>{}, 1);
#pragma unroll 1
    for (int gi = 2; gi < 8; ++gi) kgroup(IC<0>{}, gi);
    STAMP(3);
    const float mw = wave_max(fmaxf(fmaxf(fmaxf(sv[0], sv[1]), fmaxf(sv[2], sv[3])), fmaxf(fmaxf(sv[4], sv[5]), fmaxf(sv[6], sv[7]))));
    unsigned pp[8];
    {
      float lw = 0.f;
      FOR(I, 8) { float e = __builtin_amdgcn_exp2f((sv[I] - mw) * C1); pp[I] = pack2(e, e); lw += bflo(pp[I]); }
      lw = wave_sum(lw);
      if (lane == 0) {
        *(volatile LAS f32x2*)(sm + L_ML + w * 8) = f32x2{mw, lw};
        __hip_atomic_fetch_add((LAS unsigned*)(sm + L_CNT), 1u, __ATOMIC_RELAXED, __HIP_MEMORY_SCOPE_WORKGROUP);
      }
    }
    f32x4 acc = {0.f, 0.f, 0.f, 0.f};
    auto vgroup = [&](int gi) {
      sfor<16>([&](auto J) {
        unsigned pj = dppu<NBC + J>(pp[0]);
        u32x4 a;
        FOR(I, 4) { a[I] = pj & msk[I]; }
        ctl(J);
        vstepm<(J & 3) * 1024>(acc, a, buf[J], voff, vb + piece(gi * 4 + 4 + (J >> 2)), md);
      });
      FOR(I, 7) pp[I] = pp[I + 1];
    };
#pragma unroll 1
    for (int gi = 0; gi < 7; ++gi) vgroup(gi);
    float fw;
    int side;
    {
      volatile LAS unsigned* fin = (volatile LAS unsigned*)(sm + L_FIN);
      _Pragma("nounroll") for (int it = 0; it < SPIN_MAX * 4 && fin[2] == 0; ++it) __builtin_amdgcn_s_sleep(2);
      side = __builtin_amdgcn_readfirstlane(fin[2]) & 8192;
      fw = __builtin_amdgcn_exp2f((mw - __uint_as_float(fin[0])) * C1) * __uint_as_float(fin[1]);
      pin(fw);
    }
    sfor<16>([&](auto J) {
      unsigned pj = dppu<NBC + J>(pp[0]);
      u32x4 a;
      FOR(I, 4) { a[I] = pj & msk[I]; }
      vlast<15 - J>(acc, a, buf[J]);
    });
    asm volatile("s_nop 7" : "+v"(acc));
    if (lane < 32) *(LAS f32x4*)(sm + L_OP + w * 512 + c * 32 + r * 16) = acc * fw;
    STAMP(16);
    if (GL && (isG | isD)) {
      asm volatile("" ::: "memory");
      if (lane == 0) __hip_atomic_fetch_add((LAS unsigned*)(sm + L_FIN + 12), 1u, __ATOMIC_RELAXED, __HIP_MEMORY_SCOPE_WORKGROUP);
    } else {
      barrier();
    }
    if (GD0) __builtin_amdgcn_s_sleep(GD0);
    auto gathered = [&]() {
      _Pragma("nounroll") for (int it = 0; it < SPIN_MAX * 4 && *(volatile LAS unsigned*)(sm + L_CNT + 12) == 0; ++it) __builtin_amdgcn_s_sleep(1);
      asm volatile("" ::: "memory");
    };
    const unsigned idx = (c & 1) ? 0xEEEEu : 0x4444u;
    int cnt = SPIN_MAX;
    ASTDECL;
    if (isG) {
      const char* yb = ws + WS_Y + (side ^ NPY) + w * 512;
      const unsigned ya = (lane >> 5) * 2048 + (lane & 31) * 16 + 4096, yc = ya + 16384;
      u32x32 y;
#define YLD(r, a, o) "global_load_dwordx4 v[" r "], %[" a "], %[yb] offset:" o " sc1\n\t"
#define YLD8 YLD("128:131", "ya", "-4096") YLD("132:135", "ya", "0") YLD("136:139", "yc", "-4096") YLD("140:143", "yc", "0")
      asm volatile("" : "={v[128:159]}"(y));
      gathered();
      LAS char* p = sm + L_G + (r * 4 + (c >> 2)) * 128 + (c & 2) * 32 + ((w ^ ((r * 2 + (c >> 3)) & 3)) * 16) + (c & 1) * 8;
      const u32x32 f0 = tr_frags<128, 4>(p, 8192), f1 = tr_frags<128, 4>(p + 32768, 8192);
      u32x16 tmp;
      u64 any;
#define YRL(x0, x1, x2, x3, a, o) "v_max3_u32 v160, v" x0 ", v" x1 ", v" x2 "\n\tv_max_u32 v160, v160, v" x3 "\n\tv_cmp_eq_u32 vcc, -1, v160\n\t" \
                                  "s_or_b64 %[any], %[any], vcc\n\ts_mov_b64 exec, vcc\n\t" YLD(x0 ":" x3, a, o) "s_mov_b64 exec, -1\n\t"
#define GST(n, a, f) "s_waitcnt lgkmcnt(" n ")\n\t" SMF("164:167", a, f)
#define DOT(h) "v_dot2c_f32_bf16 v168, 0x3f803f80, " h "\n\tv_dot2c_f32_bf16 v169, " h ", " h "\n\t"
#define RED(m) "v_add_f32_dpp v168, v168, v168 " m " bank_mask:0xf\n\tv_add_f32_dpp v169, v169, v169 " m " bank_mask:0xf\n\t"
      asm volatile(
          "v_mov_b32 v160, 0x3f803f80\n\tv_mov_b32 v161, v160\n\tv_mov_b32 v162, v160\n\tv_mov_b32 v163, v160\n\t"
          "v_mov_b32 v164, 0\n\tv_mov_b32 v165, 0\n\tv_mov_b32 v166, 0\n\tv_mov_b32 v167, 0\n\tv_mov_b32 v168, 0\n\tv_mov_b32 v169, 0\n\t"
          SMF("164:167", "160:163", "64:71") SMF("164:167", "160:163", "72:79") SMF("164:167", "160:163", "80:87") SMF("164:167", "160:163", "88:95")
          SMF("164:167", "160:163", "96:103") SMF("164:167", "160:163", "104:111") SMF("164:167", "160:163", "112:119") SMF("164:167", "160:163", "120:127")
          "s_nop 7\n\tv_add_f32 v170, v164, v165\n\t"
          "v_mov_b32 v164, 0\n\tv_mov_b32 v165, 0\n\tv_mov_b32 v166, 0\n\tv_mov_b32 v167, 0\n\t"
          "s_barrier\n\t" SLEEP(YD0) YLD8
          "\nLY%=:\n\ts_waitcnt vmcnt(0)\n\t"
          PKA("144:145", "128:129", "132:133") PKA("146:147", "130:131", "134:135") PKA("148:149", "136:137", "140:141") PKA("150:151", "138:139", "142:143")
          PKA("144:145", "144:145", "148:149") PKA("146:147", "146:147", "150:151") "s_nop 1\n\t"
          "v_permlane32_swap_b32 v144, v146\n\tv_permlane32_swap_b32 v145, v147\n\ts_nop 0\n\t"
          PKA("144:145", "144:145", "146:147") "s_nop 0\n\tv_pk_add_f32 v[144:145], v[144:145], %[xf]\n\ts_nop 0\n\t"
          "v_cmp_u_f32 vcc, v144, v145\n\tv_cvt_pk_bf16_f32 v171, v144, v145\n\ts_cbranch_vccz LH%=\n\t"
          "s_mov_b64 %[any], 0\n\t"
          YRL("128", "129", "130", "131", "ya", "-4096") YRL("132", "133", "134", "135", "ya", "0")
          YRL("136", "137", "138", "139", "yc", "-4096") YRL("140", "141", "142", "143", "yc", "0")
          SLEEP(PB2)
          "s_cmp_eq_u64 %[any], 0\n\ts_cbranch_scc1 LT%=\n\ts_sub_u32 %[cnt], %[cnt], 1\n\ts_cbranch_scc0 LY%=\n"
          "LT%=:\n\ts_waitcnt vmcnt(0)\n"
          "LH%=:\n\t" AST(0)
          "ds_write_b32 %[hw], v171\n\ts_waitcnt lgkmcnt(0)\n\ts_barrier\n\t"
          "ds_read_b128 v[128:131], %[ha]\n\tds_read_b128 v[160:163], %[hs]\n\tds_read_b128 v[132:135], %[ha] offset:128\n\t"
          "ds_read_b128 v[136:139], %[ha] offset:256\n\tds_read_b128 v[140:143], %[ha] offset:384\n\tds_read_b128 v[144:147], %[ha] offset:512\n\t"
          "ds_read_b128 v[148:151], %[ha] offset:640\n\tds_read_b128 v[152:155], %[ha] offset:768\n\tds_read_b128 v[156:159], %[ha] offset:896\n\t"
          GST("8", "128:131", "64:71") "s_waitcnt lgkmcnt(7)\n\t" DOT("v160") DOT("v161")
          GST("6", "132:135", "72:79") DOT("v162") DOT("v163")
          GST("5", "136:139", "80:87") RED("row_ror:8 row_mask:0xf")
          GST("4", "140:143", "88:95") RED("row_ror:4 row_mask:0xf")
          GST("3", "144:147", "96:103") RED("row_ror:2 row_mask:0xf")
          GST("2", "148:151", "104:111") RED("row_ror:1 row_mask:0xf")
          GST("1", "152:155", "112:119") RED("row_bcast:15 row_mask:0xa")
          GST("0", "156:159", "120:127") RED("row_bcast:31 row_mask:0xc")
          "v_mul_f32 v168, 0xbb000000, v168\n\t"
          "v_fma_f32 v169, v169, %[k], %[eps]\n\t"
          "v_fma_f32 v169, -v168, v168, v169\n\t"
          "v_max_f32 v169, v169, %[eps]\n\t"
          "v_rsq_f32 v169, v169\n\t"
          "v_mul_f32 v168, v168, v170\n\t"
          "v_add_f32 v164, v164, v165\n\t"
          "v_mul_f32 v170, 0xbfb8aa3b, v169\n\t"
          "v_add_f32 v164, v164, v168\n\t"
          "v_mul_f32 v165, v164, v169\n\t"
          "v_mul_f32 v164, v164, v170\n\t"
          "v_exp_f32 v164, v164\n\t"
          "v_mul_f32_dpp v165, v165, v165 row_ror:8 row_mask:0xf bank_mask:0xf\n\t"
          "v_add_f32 v164, 1.0, v164\n\t"
          "v_rcp_f32 v164, v164\n\t"
          "s_nop 0\n\t"
          "v_mul_f32 v164, v164, v165\n\t"
          "s_nop 1\n\t"
          "v_mov_b32_dpp v165, v164 quad_perm:[1,0,3,2] row_mask:0xf bank_mask:0xf\n\t"
          "v_cvt_pk_bf16_f32 v164, v164, v165\n\t"
          "s_mov_b32 exec_lo, 0\n\ts_mov_b32 exec_hi, 0x550000\n\t"
          "v_min_u32 v164, -2, v164\n\t"
          "global_store_dword %[aa], v164, %[ab] sc1\n\t"
          "s_mov_b64 exec, -1\n" KEND0 AST(1) ASTEND
          : "+{v[128:159]}"(y), "=&{v[160:175]}"(tmp), [any] "=&s"(any), [cnt] "+s"(cnt) ASTOUT
          : "{v[64:95]}"(f0), "{v[96:127]}"(f1), [xf] "v"(f32x2{bflo(xg), bfhi(xg)}), [ya] "v"(ya), [yc] "v"(yc), [yb] "s"(yb), [idx] "v"(idx),
            [hw] "v"(sm + L_H + hoff), [ha] "v"(sm + L_H + (c & 1) * 64 + r * 16), [hs] "v"(sm + L_H + lane * 16),
            [k] "v"(1.f / DM), [eps] "v"(1e-5f), [aa] "v"(c * 2), [ab] "s"(ws + WS_ACT + (side ^ NPA) + gidx * 64 + w * 16)
          : "memory", "vcc", "scc");
      ASTPUT(10, 0); ASTPUT(11, 1); ACNT(20, SPIN_MAX - cnt + 1);
    } else if (isD) {
      const char* ab = ws + WS_ACT + (side ^ NPA) + w * 896;
      const unsigned ao = min(c >> 1, 6) * 128 + r * 32 + (c & 1) * 16;
      u32x32 av;
      asm volatile("" : "={v[128:159]}"(av));
      gathered();
      LAS char* p = sm + L_G + w * 14336 + r * 128 + c * 8;
      const u32x32 f0 = tr_frags<32, 4>(p, 2048), f1 = tr_frags<32, 3>(p + 8192, 2048);
      float t, u;
#define Z4(a, b) "v_mov_b64 v[" a "], 0\n\tv_mov_b64 v[" b "], 0\n\t"
#define RADD(d, a, b, m) "v_add_f32_dpp %[" d "], v" a ", v" b " quad_perm:[0,1,2,3] row_mask:" m " bank_mask:0xf\n\t"
      asm volatile(
          Z4("132:133", "134:135") Z4("136:137", "138:139") Z4("140:141", "142:143") Z4("144:145", "146:147") Z4("148:149", "150:151")
          Z4("152:153", "154:155") Z4("156:157", "158:159") "v_mov_b32 %[u], 0\n\t"
          "s_barrier\n\t" SLEEP(AD0) "global_load_dwordx4 v[128:131], %[ao], %[ab] sc1\n"
          "\nLA%=:\n\ts_waitcnt vmcnt(0)\n\t"
          "v_max3_u32 %[t], v128, v129, v130\n\tv_max_u32 %[t], %[t], v131\n\tv_cmp_eq_u32 vcc, -1, %[t]\n\ts_cbranch_vccz LD%=\n\t"
          SLEEP(PB3)
          "s_mov_b64 exec, vcc\n\tglobal_load_dwordx4 v[128:131], %[ao], %[ab] sc1\n\ts_mov_b64 exec, -1\n\t"
          "s_sub_u32 %[cnt], %[cnt], 1\n\ts_cbranch_scc0 LA%=\n\ts_waitcnt vmcnt(0)\n"
          "LD%=:\n\t" AST(0)
          SMF("132:135", "128:131", "64:71") SMF("136:139", "128:131", "72:79") SMF("140:143", "128:131", "80:87") SMF("144:147", "128:131", "88:95")
          SMF("148:151", "128:131", "96:103") SMF("152:155", "128:131", "104:111") SMF("156:159", "128:131", "112:119")
          "s_nop 1\n\t"
          RADD("t", "132", "133", "0x1") RADD("u", "138", "139", "0x1") RADD("t", "140", "141", "0x2") RADD("u", "146", "147", "0x2")
          RADD("t", "148", "149", "0x4") RADD("u", "154", "155", "0x4") RADD("t", "156", "157", "0x8")
          "v_add_f32 %[t], %[t], %[u]\n\tds_write_b32 %[dw], %[t]\n\t" AST(1) "s_waitcnt lgkmcnt(0)\n\ts_barrier"
          : "+{v[128:159]}"(av), [t] "=&v"(t), [u] "=&v"(u), [cnt] "+s"(cnt) ASTOUT
          : "{v[64:95]}"(f0), "{v[96:127]}"(f1), [ao] "v"(ao), [ab] "s"(ab), [idx] "v"(idx), [dw] "v"(sm + L_DP + c * 64 + r * 16 + w * 4)
          : "memory", "vcc", "scc");
      ASTPUT(11, 0); ACNT(20, SPIN_MAX - cnt + 1);
    }
    if (w == 0) STAMP_OUT();
  } else if (w == 5) {
    const unsigned lds0 = (unsigned)(uintptr_t)sm;
    const int hr = (lane >> 4) * 16 + ((lane >> 1) & 7), hf = lane & 1;
    auto blk = [](int i) { return (size_t)((i >> 1) * 65536 + (i & 1) * 8192); };
    const char* srcW = wo + (size_t)(g * 512 + hr) * 1024 + j * 32 + hf * 16;
    auto touch = [&]() {
      const int k = lane >> 1;
      const char* p = k == 0 ? out : k == 1 ? vc + (size_t)(cb >> LG) * (524288 << LG) : (k == 2 ? wg : k == 3 ? wu : wd) + (lane & 1) * (DM * FF * 2 - 16);
      if (lane < (PRE > 1 && (isG | isD | (g >> 1 == 1)) ? 10 : 4)) dma(p, lds0 + L_G);
    };
    if (PRE == 3) touch();
    barrier();
    if (CPF0) asm volatile("s_waitcnt vmcnt(0)" : "+v"(cp0), "+v"(cp1), "+v"(cp2) : : "memory");
    if (PRE < 3) touch();
    int due = 0, tk = 0;
    const int unit = isG && !GL ? TG * 256 / 192 : TG * 256 / 64;
    auto pace = [&](int cost) {
      _Pragma("nounroll") for (int it = 0; it < SPIN_MAX * 4 && tk < due; ++it) {
        tk = __builtin_amdgcn_readfirstlane(*(volatile LAS unsigned*)(sm + L_PROG)) * 256;
        if (tk < due) __builtin_amdgcn_s_sleep(3);
      }
      due += cost * unit;
    };
    auto dw = [&](int s_) { const int i = WROT == 1 ? (s_ + (j & 3) * 4) & 15 : WROT == 2 ? (s_ & ~3) | ((s_ + j) & 3) : s_; dma(srcW + blk(i), lds0 + L_W + i * 1024); };
    auto wdone = [&]() { if (lane == 0) *(volatile LAS unsigned*)(sm + L_CNT + 4) = 1; };
    auto tag = [&]() {
      _Pragma("nounroll") for (int it = 0; it < SPIN_MAX * 4 && *(volatile LAS unsigned*)(sm + L_CNT + 8) == 0; ++it) __builtin_amdgcn_s_sleep(4);
      asm volatile("" ::: "memory");
      return *(volatile LAS u32x2*)(sm + L_EP + 8);
    };
    if (b == 7) {
      __builtin_amdgcn_s_sleep(PRD);
      unsigned m0 = ~0u, m1 = ~0u;
      _Pragma("nounroll") for (int i = 0; i < 1024; i += 128) {
        u64 t0, t1, t2; unsigned v;
        asm volatile("s_memtime %0\n\ts_waitcnt lgkmcnt(0)\n\ts_load_dword %3, %4, %5\n\ts_waitcnt lgkmcnt(0)\n\ts_memtime %1\n\ts_waitcnt lgkmcnt(0)\n\t"
                     "s_load_dword %3, %4, %6\n\ts_waitcnt lgkmcnt(0)\n\ts_memtime %2\n\ts_waitcnt lgkmcnt(0)"
                     : "=&s"(t0), "=&s"(t1), "=&s"(t2), "=&s"(v) : "s"(ws + WS_PRB), "s"(i), "s"(i + 8192) : "memory");
        m0 = min(m0, (unsigned)(t1 - t0)); m1 = min(m1, (unsigned)(t2 - t1));
      }
      const u32x2 e = tag();
      if (lane == 0) st2_sc1(0, ws + WS_SIDE, u32x2{e[0], e[1] ^ (m0 < m1 ? 1u : 0u)});
    }
    if (C3 && g == 2 + (q3 >> 2) && j < 16) {
      _Pragma("nounroll") for (int n = j * 14; n < j * 14 + 14; n += 2) {
        u32x4 t0, t1;
        const unsigned a = (n * 8 + (lane >> 3)) * 1024 + q3 * 128 + (lane & 7) * 16;
        asm volatile("global_load_dwordx4 %0, %2, %4 nt\n\tglobal_load_dwordx4 %1, %3, %4 nt\n\ts_waitcnt vmcnt(0)" : "=&v"(t0), "=&v"(t1) : "v"(a), "v"(a + 8192), "s"(wd) : "memory");
        const unsigned o = ((lane >> 1) & 3) * 57344 + (n >> 3) * 2048 + ((n >> 1) & 3) * 256 + (lane >> 3) * 32 + (lane & 1) * 16;
        if (C3 == 1) { st4_sc1(o, ws + WS_C3, t0); st4_sc1(o + 1024, ws + WS_C3, t1); }
        if (C3 == 3) asm volatile("global_store_dwordx4 %0, %1, %3\n\tglobal_store_dwordx4 %0, %2, %3 offset:1024\n\ts_nop 1" ::"v"(o), "v"(t0), "v"(t1), "s"(ws + WS_C3) : "memory");
        if (C3 == 4) asm volatile("global_store_dwordx4 %0, %1, %3 sc0 sc1\n\tglobal_store_dwordx4 %0, %2, %3 offset:1024 sc0 sc1\n\ts_nop 1" ::"v"(o), "v"(t0), "v"(t1), "s"(ws + WS_C3) : "memory");
        if (C3 == 5) asm volatile("global_store_dwordx4 %0, %1, %3 sc1 nt\n\tglobal_store_dwordx4 %0, %2, %3 offset:1024 sc1 nt\n\ts_nop 1" ::"v"(o), "v"(t0), "v"(t1), "s"(ws + WS_C3) : "memory");
      }
      const u32x2 e = tag();
      waitv<0>();
      if (lane == 0) st2_sc1(j * 8, ws + WS_CF, e);
    }
    if (isC) {
      _Pragma("nounroll") for (int i = 0; i < 16; ++i) { dw(i); waitv<1>(); }
      waitv<0>();
      wdone();
      const u32x2 e = tag();
      _Pragma("nounroll") for (int it = 0; it < SPIN_MAX; ++it) {
        u32x2 f;
        asm volatile("global_load_dwordx2 %0, %1, %2 sc1\n\ts_waitcnt vmcnt(0)" : "=&v"(f) : "v"((unsigned)((lane & 15) * 8)), "s"(ws + WS_CF) : "memory");
        if (!__any(f[0] != e[0] || f[1] != e[1])) break;
        __builtin_amdgcn_s_sleep(32);
      }
      const char* src = ws + WS_C3 + (didx & 3) * 57344 + lane * 16;
      _Pragma("nounroll") for (int i = 0; i < 56; ++i) { dma_sc1(src + i * 1024, lds0 + L_G + i * 1024); waitv<CWIN>(); }
    } else if (DSPL && isD) {
      const char* src = wd + (size_t)hr * 1024 + didx * 32 + hf * 16;
      const int part = lane * DSPL >> 6;
      auto piecewise = [&](const char* a, unsigned dst) {
        FOR(Q, DSPL) { if (part == Q) dma(a, dst); waitv<GWD>(); if (DSL) __builtin_amdgcn_s_sleep(DSL); }
      };
      _Pragma("nounroll") for (int m = 0; m < 28; ++m) {
        piecewise(src + blk(2 * m), lds0 + L_G + m * 2048);
        piecewise(src + blk(2 * m + 1), lds0 + L_G + m * 2048 + 1024);
        if (m < 16) piecewise(srcW + blk(m), lds0 + L_W + m * 1024);
        if (m == 18) { waitv<(DSPL ? 2 * DSPL : 1)>(); wdone(); }
      }
    } else if (DPF && (g >> 1) == 1) {
      const char* src = isD ? wd + (size_t)hr * 1024 + didx * 32 + hf * 16 : wd + (size_t)(j * 8 + ((lane >> 2) & 7)) * 1024 + (g & 1) * 512 + (lane & 3) * 128;
      _Pragma("nounroll") for (int t = 0, wi = 0; t < (isD ? 56 + DLAG : 53); ++t) {
        due = t * (TD * 256 / 56);
        pace(0);
        if (isD) { if (t >= DLAG) dma(src + blk(t - DLAG), lds0 + L_G + (t - DLAG) * 1024); }
        else if ((t & 3) == 0 && lane < 32) dma(src + (size_t)t * 32768, lds0 + L_G);
        if (t == wi * 3 + 1 && wi < 16) dw(wi++);
        waitv<GWD>();
        if (t == 50) { waitv<1>(); wdone(); }
      }
    } else
    if (isD) {
      const char* src = wd + (size_t)hr * 1024 + didx * 32 + hf * 16;
      auto dd = [&](int s_) {
        int i = s_;
        if (DROT == 1) { i += (j & 3) * 14; i = i >= 56 ? i - 56 : i; }
        if (DROT == 2) i = (i & ~7) | ((i + (j & 3) * 2) & 7);
        if (DROT == 3) i = (i & ~3) | ((i + j) & 3);
        if (DROT == 4) dma(wd + (size_t)((lane >> 4) * 448 + ((lane >> 1) & 7) + i * 8) * 1024 + didx * 32 + hf * 16, lds0 + L_G + i * 1024);
        else if (DROT == 5) dma(wd + (size_t)((lane >> 1) * 56 + i) * 1024 + didx * 32 + hf * 16, lds0 + L_G + i * 1024);
        else if (DROT == 6) dma(wd + (size_t)((lane >> 1) + i * 32) * 1024 + didx * 32 + hf * 16, lds0 + L_G + i * 1024);
        else if (DROT == 7) dma(wd + (size_t)((lane >> 1) * 2 + (i & 1) + (i >> 1) * 64) * 1024 + didx * 32 + hf * 16, lds0 + L_G + i * 1024);
        else
        dma(src + blk(i), lds0 + L_G + i * 1024);
      };
      _Pragma("nounroll") for (int m = 0; m < 17; ++m) {
        dd(2 * m); waitv<GWD + 1>(); dd(2 * m + 1); waitv<GWD>();
        if (m < 16) dw(m);
      }
      if (GWD > 1) waitv<1>();
      wdone();
      asm volatile(KEND1);
      _Pragma("nounroll") for (int i = 34; i < 56; ++i) { dd(i); waitv<GWD>(); }
    } else {
      _Pragma("nounroll") for (int i = 0; i < 16; ++i) {
        pace(4);
        dw(i); waitv<GWIN>();
      }
      waitv<0>();
      wdone();
      if (isG) {
        const char* src = ((lane & 4) ? wu : wg) + (size_t)(lane >> 3) * (FF * 2) + gidx * 64 + ((lane ^ (lane >> 4)) & 3) * 16;
        _Pragma("nounroll") for (int s_ = 0; s_ < 64; ++s_) { const int i = s_ ^ ((j & 1) * GROT); pace(2); dma(src + (size_t)i * (8 * FF * 2), lds0 + L_G + i * 1024); waitv<GWG>(); }
      }
    }
    waitv<0>();
    STAMP(28);
    if (lane == 0) *(volatile LAS unsigned*)(sm + L_CNT + 12) = 1;
    STAMP_OUT();
  } else {
    STAMPR(0); STAMP(1);
    u32x2 ep;
    if (!EPF) asm volatile("global_load_dwordx2 %0, %1, %2 sc1" : "=v"(ep) : "v"(0u), "s"(ws + WS_EPOCH) : "memory");
    const u32x4 sent4 = {SENT, SENT, SENT, SENT};
    const char* mlb = ws + WS_ML + head * 128;
    const char* pob = ws + WS_PO + (g & 3) * 16384 + j * 256;
    const char* yb = ws + WS_Y + (g & 3) * 2048 + (g >> 2) * 16384 + j * 64;
    int side = 0;
    if (lane < 16) st4_sc1(lane * 16 + ((g & 4) << 11), pob, sent4);
    if (lane < 8) st4_sc1((lane & 3) * 16 + (lane >> 2) * 8192, yb, sent4);
    if (lane < NB && (j & 3) == 1) st1_sc1(lane * 1024 + (cb >> 2) * 16, ws + WS_ROW, SENT);
    if (lane == 0) st2_sc1(split * 8, mlb, u32x2{SENT, SENT});
    if (b == 6 && lane < 16) st1_sc1((lane & 1) * 8192 + (lane >> 1) * 128, ws + WS_PRB, 0u);
    if (lane < 8 && isG) st4_sc1((lane & 3) * 16 + (lane >> 2) * 8192, ws + WS_ACT + gidx * 64, sent4);
    if (lane < 4 && isG && gidx < 32) st4_sc1((lane & 1) * 16 + (lane >> 1) * 8192, ws + WS_H + gidx * 128, sent4);
    if (lane == 0) { *(LAS u32x4*)(sm + L_CNT) = u32x4{0, 0, 0, 0}; *(LAS u32x4*)(sm + L_FIN) = u32x4{0, 0, 0, 0}; }
    if (lane < 4) { *(LAS unsigned*)(sm + L_MODE + lane * 4) = WB4 / 4 - 4; *(LAS unsigned*)(sm + L_PROG + lane * 256) = 0; }
    barrier();
    f32x2 qp;
    {
      f32x2 t[4];
      if (EPF) ep = *(LAS u32x2*)(sm + L_EP);
      FOR(I, 4) { t[I] = *(LAS f32x2*)(sm + L_QP + I * 512 + lane * 8); }
      qp = (t[0] + t[1]) + (t[2] + t[3]);
    }
    if (!EPF) waitv<0>(ep);
    const u64 E = (((u64)ep[1] << 32) | ep[0]) * 6364136223846793005ull + 1442695040888963407ull;
    const u32x2 e2 = {(unsigned)E, (unsigned)(E >> 32)};
    st4_sc1(lane * 16, ws + WS_Q + cb * 1024, u32x4{__float_as_uint(qp[0]), e2[0], __float_as_uint(qp[1]), e2[1]});
    STAMP(23);
    if (EPF) waitv<1>();
    if (lane == 0) st2_sc1(b * 8, ws + WS_READY, e2);
    auto rfl = [](unsigned v) { return (unsigned)__builtin_amdgcn_readfirstlane(v); };
    unsigned brt = rfl((unsigned)__builtin_amdgcn_s_memrealtime()), bct = rfl((unsigned)__builtin_amdgcn_s_memtime());
    if (b == 0 && lane == 0) st4_sc1(2048, ws + WS_READY, u32x4{e2[0], e2[1], brt, 0u});
    {
      __builtin_amdgcn_s_sleep(QD0);
      u32x4 t[8];
      _Pragma("nounroll") for (int it = 0; it < SPIN_MAX; ++it) {
        sfor<8>([&](auto T) { ld_sc1<(T & 3) * 1024>(t[T], lane * 16 + (T >> 2) * 4096, ws + WS_Q + head * 8192); });
        unsigned bad = 0;
        sfor<8>([&](auto T) { waitv<7 - T>(t[T]); bad |= (t[T][1] ^ e2[0]) | (t[T][3] ^ e2[1]); });
        CNT(7);
        if (!__any(bad != 0)) break;
        __builtin_amdgcn_s_sleep(QD1);
      }
      auto S = [&](int i) { auto F = [&](int k) { return __uint_as_float(t[k][i]); }; return ((F(0) + F(1)) + (F(2) + F(3))) + ((F(4) + F(5)) + (F(6) + F(7))); };
      *(volatile LAS unsigned*)(sm + L_QB + lane * 4) = pack2(S(0), S(2));
      *(volatile LAS u32x2*)(sm + L_EP + 8) = e2;
      asm volatile("s_waitcnt lgkmcnt(0)" ::: "memory");
      if (lane == 0) *(volatile LAS unsigned*)(sm + L_CNT + 8) = 1;
    }
    STAMP(15);
    u32x32 f0, f1;
    const int h0 = (j >> 3) + 1;
    {
      int mls = 0, rdy = 0, kb_ = 0, pend = 0;
      unsigned tm = brt, tr = brt + RDY0, tb = 0;
      float M = 0.f, Li = 0.f;
      const int ff = FFW ? (int)((isC ? UC : isD ? UD : isG ? UG : 0.f) * 4) : 0;
      float cu = FFW ? 0.f : isC ? UC : isD ? UD : isG ? UG : 0.f, cs = cu * TSH, lag0 = 0.f, lead = 0.f;
      int w16 = WB4 * 4 + (int)(cu * 4.f);
      float clk = 1.f / 23.4f;
      _Pragma("nounroll") for (int it = 0; it < SPIN_MAX * 4; ++it) {
        const unsigned pw = *(volatile LAS unsigned*)(sm + L_PROG + (lane & 3) * 256);
        const u32x4 cn = *(volatile LAS u32x4*)(sm + L_CNT);
        const unsigned ct = rfl((unsigned)__builtin_amdgcn_s_memtime());
        unsigned now = brt + rfl((unsigned)((float)(ct - bct) * clk));
        unsigned ps = pw + dppu<QX1>(pw); ps += dppu<QX2>(ps);
        const int p = rfl(ps), cnt = rfl(cn[0]), gd = rfl(cn[1]);
        {
          const int eb = p >= min(PBS - KB * (w16 - WB4 * 4) / 16, PEND) ? EB : 0;
          const int wl = min(max(w16 + eb + (FFW && cn[3] == 0 ? ff : 0) + KW * (int)(ps - 4 * pw), 64), WMX);
          if (lane < 4) *(volatile LAS unsigned*)(sm + L_MODE + lane * 4) = ((wl + ((lane * 4 + it * 5) & 15)) >> 4) - 4;
        }
        if (!rdy && (int)(now - tr) >= 0) {
          u32x4 ra, rb, rc;
          ld_sc1<0>(ra, lane * 32, ws + WS_READY); ld_sc1<16>(rb, lane * 32, ws + WS_READY); ld_sc1<2048>(rc, 0u, ws + WS_READY);
          waitv<0>(ra); pin(rb); pin(rc);
          bool ok = ra[0] == e2[0] && ra[1] == e2[1] && ra[2] == e2[0] && ra[3] == e2[1] && rb[0] == e2[0] && rb[1] == e2[1] && rb[2] == e2[0] && rb[3] == e2[1] &&
                    rc[0] == e2[0] && rc[1] == e2[1];
          rdy = rfl(__all(ok));
          tr = now + 200;
          tb = rfl(rc[2]) + B0;
          if (rdy && b == 0 && lane == 0) st2_sc1(0, ws + WS_EPOCH, e2);
        } else if (rdy && !pend && kb_ < NB && (int)(now - tb) >= 0) {
          now = rfl((unsigned)__builtin_amdgcn_s_memrealtime());
          clk = (float)(now - brt) * __builtin_amdgcn_rcpf((float)(ct - bct));
          brt = now; bct = ct;
          const int fp = (p < 128 ? p * TK : 128 * TK + (p - 128) * TV) - (int)(now - tb);
          if (lane == 0 && (j & 3) == 1) st2_sc1(kb_ * 1024 + (cb >> 2) * 16, ws + WS_ROW, u32x2{(unsigned)(fp + 65536), __float_as_uint(cu)});
          lead = (float)fp;
          pend = 1; tr = tb + (j == kb_ * 4 ? RD1 : RD2);
        } else if (pend && (int)(now - tr) >= 0) {
          u32x2 r2;
          asm volatile("global_load_dwordx2 %0, %1, %2 sc1\n\ts_waitcnt vmcnt(0)" : "=&v"(r2) : "v"(lane * 16), "s"(ws + WS_ROW + kb_ * 1024) : "memory");
          const unsigned rv = r2[0];
          const float su = wave_sum(rv != SENT ? (float)rv : 0.f), n = wave_sum(rv != SENT ? 1.f : 0.f);
          const float mean = su * __builtin_amdgcn_rcpf(n) - 65536.f, lag = mean - lead, dt = (float)(int)(now - tb) + 80.f;
          if (kb_ == 0) lag0 = n > 40.f ? lag : 0.f;
          const float dist = (lag - lag0 + cs * (1.f / GI)) * __builtin_amdgcn_rcpf((float)(kb_ * DB + TSH));
          const float togo = fmaxf((float)(128 * TK + (PTG - 128) * TV) - mean - dt, (float)HMIN);
          cs = fmaf(cu, dt, cs);
          const float un = GI * (dist + (lag + (dist - cu * (1.f / GI)) * dt) * __builtin_amdgcn_rcpf(togo));
          if (n > 40.f) cu = fminf(fmaxf(un, (float)(WLO - WB4)), (float)(WHI - WB4));
          cs = fmaf(cu, DB - dt, cs);
          w16 = WB4 * 4 + (int)(cu * 4.f);
          pend = 0; ++kb_; tb += DB;
          if (!GL) ACC(29, w16);
        } else if (mls == 0 && rdy && cnt == 4) {
          f32x2 ml = *(volatile LAS f32x2*)(sm + L_ML + (lane & 3) * 8);
          float Mw = row_max(ml[0]);
          float L = ml[1] * __builtin_amdgcn_exp2f((ml[0] - Mw) * C1);
          L += dppf<QX1>(L); L += dppf<QX2>(L);
          if (lane == 0) st2_sc1(split * 8, mlb, u32x2{canon(__float_as_uint(Mw)), canon(__float_as_uint(L))});
          mls = 1; tm = now + MLD;
        } else if (mls == 1 && (int)(now - tm) >= 0) {
          u32x2 o;
          asm volatile("global_load_dwordx2 %0, %1, %2 sc1\n\ts_waitcnt vmcnt(0)" : "=&v"(o) : "v"((unsigned)(lane < 32 ? WS_ML + head * 128 + (lane & 7) * 8 : WS_SIDE)), "s"(ws) : "memory");
          tm = now + 150;
          if (!rfl(__any(lane < 32 ? o[0] == SENT || o[1] == SENT : o[0] != e2[0] || (o[1] ^ e2[1]) > 1u))) {
            side = ((__builtin_amdgcn_readlane(o[1] ^ e2[1], 32) & 1) << 13) ^ NPS;
            float m2 = __uint_as_float(o[0]);
            M = row_max(m2);
            float L = __uint_as_float(o[1]) * __builtin_amdgcn_exp2f((m2 - M) * C1);
            L += dppf<QX1>(L); L += dppf<QX2>(L); L += dppf<HMIR>(L);
            Li = 1.f / L;
            mls = 2;
          }
        } else if (mls == 2) {
          if (lane == 0) {
            volatile LAS unsigned* fin = (volatile LAS unsigned*)(sm + L_FIN);
            fin[0] = __float_as_uint(M); fin[1] = __float_as_uint(Li); fin[2] = side | 1;
          }
          mls = 3;
          STAMP(22);
        } else if (PWARM && mls == 3 && p >= PWARM) {
          unsigned t;
          asm volatile("global_load_dword %0, %1, %2 sc1\n\ts_waitcnt vmcnt(0)" : "=&v"(t) : "v"((unsigned)(lane < 2 ? WS_PO + cb * 256 + lane * 128 : !isG || lane < 16 ? WS_Y + g * 2048 + j * 64 : lane < 32 ? WS_ACT + gidx * 64 : WS_H + (gidx & 31) * 128)), "s"(ws) : "memory");
          mls = 4;
        } else if (mls == (PWARM ? 4 : 3) && gd && p >= PEND) {
          break;
        } else {
          __builtin_amdgcn_s_sleep(SLP);
        }
      }
      if (CPF1) code_touch(cp0, cp1, cp2, lane);
      auto frags = [&](int h) {
        u32x32 f;
        FOR(T, 4) { const u32x8 t = tr_frag<32>(sm + L_W + (((h + (T >> 1)) & 3) * 4096 + r * 128 + c * 8) + (T & 1) * 2048); FOR(I, 8) { f[T * 8 + I] = t[I]; } }
        return f;
      };
      f0 = frags(h0); f1 = frags(h0 + 2);
    }
    unsigned po[4], pa = lane * 4, ca = c * 4, idx = (c & 1) ? 0xEEEEu : 0x4444u;
    FOR(H, 4) { po[H] = ((h0 + H) & 3) * 2048 + (c >> 1) * 256 + r * 32 + (c & 1) * 16; }
    u32x16 d = 0;
    LAS char* op = sm + L_OP + lane * 8;
    const char* pb = ws + WS_PO + (g & 3) * 16384 + (side ^ NPO ^ ((g & 4) << 11));
    pob = pb + j * 256; yb += side ^ NPY;
    asm volatile("" : "+{v[96:111]}"(d), "+{v[112:143]}"(f0), "+{v[144:175]}"(f1), "+v"(po[0]), "+v"(po[1]), "+v"(po[2]), "+v"(po[3]), "+v"(pa), "+v"(ca), "+v"(idx), "+v"(op),
                 "+s"(pb), "+s"(pob), "+s"(yb));
    if (CPF1) asm volatile("s_waitcnt vmcnt(0)" : "+v"(cp0), "+v"(cp1), "+v"(cp2) : : "memory");
    STAMP(4);
    if (GL && (isG | isD)) {
      int cnt = SPIN_MAX * 16;
      unsigned t;
      asm volatile("LAW%=:\n\tds_read_b32 %0, %2\n\ts_sub_u32 %1, %1, 1\n\ts_cbranch_scc1 LAX%=\n\ts_waitcnt lgkmcnt(0)\n\tv_cmp_gt_u32 vcc, 4, %0\n\ts_cbranch_vccnz LAW%=\nLAX%=:\n\ts_waitcnt lgkmcnt(0)"
                   : "=&v"(t), "+s"(cnt) : "v"(sm + L_FIN + 12) : "memory", "vcc", "scc");
    } else {
      barrier();
    }
    STAMP(5);
    u32x32 a;
    {
      int todo = 15, cnt = SPIN_MAX;
      u64 msk;
      unsigned t0, t1;
      ASTDECL;
#define OLD(h, a0, a1) "global_load_dwordx4 v[" a0 "], %[po" h "], %[pb] sc1\n\tglobal_load_dwordx4 v[" a1 "], %[po" h "], %[pb] offset:128 sc1\n\t"
#define WST(l, h, a0, a1, a2, a3, a4, a5, a6, a7, d_, fa, fb) \
  "v_max3_u32 %[t0], v" a0 ", v" a1 ", v" a2 "\n\tv_max3_u32 %[t1], v" a3 ", v" a4 ", v" a5 "\n\tv_max3_u32 %[t0], %[t0], v" a6 ", v" a7 "\n\t" \
  "v_max_u32 %[t0], %[t0], %[t1]\n\tv_cmp_eq_u32 vcc, -1, %[t0]\n\ts_cbranch_vccz LC" l h "%=\n\t" \
  "s_mov_b64 %[msk], vcc\n\ts_mov_b64 exec, vcc\n\t" OLD(h, a0 ":" a3, a4 ":" a7) "s_mov_b64 exec, -1\n\ts_branch LN" l h "%=\n" \
  "LC" l h "%=:\n\t" SMF(d_, a0 ":" a3, fa) SMF(d_, a4 ":" a7, fb) "s_bitset0_b32 %[todo], " h "\nLN" l h "%=:\n\t"
#define WS0(l) WST(l, "0", "64", "65", "66", "67", "68", "69", "70", "71", "96:99", "112:119", "120:127")
#define WS1(l) WST(l, "1", "72", "73", "74", "75", "76", "77", "78", "79", "100:103", "128:135", "136:143")
#define WS2(l) WST(l, "2", "80", "81", "82", "83", "84", "85", "86", "87", "104:107", "144:151", "152:159")
#define WS3(l) WST(l, "3", "88", "89", "90", "91", "92", "93", "94", "95", "108:111", "160:167", "168:175")
#define OLE(h, a0, a1) "global_load_dwordx4 v[" a0 "], %[po" h "], %[pb] sc1 ; ovl\n\tglobal_load_dwordx4 v[" a1 "], %[po" h "], %[pb] offset:128 sc1 ; ovl\n\t"
#define WOL(h, bit, a0, a1, a2, a3, a4, a5, a6, a7, d_, fa, fb) \
  "s_cmp_eq_u32 %[todo], " bit "\n\ts_cbranch_scc0 LS" h "%=\n\t" SLEEP(WOV) \
  "LQ" h "%=:\n\ts_mov_b64 exec, %[msk]\n\t" OLE(h, a0 ":" a3, a4 ":" a7) "s_mov_b64 exec, -1\n\ts_waitcnt vmcnt(2)\n\t" \
  "v_max3_u32 %[t0], v" a0 ", v" a1 ", v" a2 "\n\tv_max3_u32 %[t1], v" a3 ", v" a4 ", v" a5 "\n\tv_max3_u32 %[t0], %[t0], v" a6 ", v" a7 "\n\t" \
  "v_max_u32 %[t0], %[t0], %[t1]\n\tv_cmp_eq_u32 vcc, -1, %[t0]\n\ts_cbranch_vccz LR" h "%=\n\ts_sub_u32 %[cnt], %[cnt], 1\n\ts_cbranch_scc0 LQ" h "%=\n" \
  "LR" h "%=:\n\t" SMF(d_, a0 ":" a3, fa) SMF(d_, a4 ":" a7, fb) "s_nop 2\n\ts_branch LE%=\nLS" h "%=:\n\t"
#define WOVL
#define WEND "s_waitcnt vmcnt(0)\n\t"
#define TODO(h) "s_bitcmp1_b32 %[todo], " h "\n\ts_cbranch_scc0 LNb" h "%=\n\t"
      asm volatile(
          "ds_read2st64_b64 v[64:67], %[op] offset1:1\n\tds_read2st64_b64 v[68:71], %[op] offset0:2 offset1:3\n\ts_waitcnt lgkmcnt(1)\n\t"
          PKA("64:65", "64:65", "66:67") "s_waitcnt lgkmcnt(0)\n\t" PKA("68:69", "68:69", "70:71") "s_nop 0\n\t" PKA("64:65", "64:65", "68:69") "s_nop 0\n\t"
          "v_cvt_pk_bf16_f32 %[t0], v64, v65\n\tv_min_u32 %[t0], -2, %[t0]\n\tglobal_store_dword %[pa], %[t0], %[pob] sc1\n\t" AST(0) SLEEP(WD0)
          OLD("0", "64:67", "68:71") OLD("1", "72:75", "76:79") OLD("2", "80:83", "84:87") OLD("3", "88:91", "92:95")
          "s_waitcnt vmcnt(6)\n\t" WS0("a") "s_waitcnt vmcnt(4)\n\t" WS1("a") "s_waitcnt vmcnt(2)\n\t" WS2("a") "s_waitcnt vmcnt(0)\n\t" WS3("a")
          "s_cmp_eq_u32 %[todo], 0\n\ts_cbranch_scc1 LE%=\n"
          "LP%=:\n\t" WOVL SLEEP(PB1) "s_waitcnt vmcnt(0)\n\t" TODO("0") WS0("b") TODO("1") WS1("b") TODO("2") WS2("b") TODO("3") WS3("b")
          "s_cmp_eq_u32 %[todo], 0\n\ts_cbranch_scc1 LE%=\n\ts_sub_u32 %[cnt], %[cnt], 1\n\ts_cbranch_scc0 LP%=\n\ts_waitcnt vmcnt(0)\n"
          "LE%=:\n\t" AST(1) WEND "s_nop 3\n\t"
          PKA("96:97", "96:97", "100:101") PKA("98:99", "98:99", "102:103") PKA("104:105", "104:105", "108:109") PKA("106:107", "106:107", "110:111")
          PKA("96:97", "96:97", "98:99") PKA("104:105", "104:105", "106:107") "s_nop 0\n\t" PKA("96:97", "96:97", "104:105") "s_nop 0\n\t"
          ROWSUM("%[t0]", "%[t1]", "v96", "v97")
          "s_mov_b64 exec, 0xffff\n\tv_min_u32 %[t0], -2, %[t0]\n\tglobal_store_dword %[ca], %[t0], %[yb] sc1\n\ts_mov_b64 exec, -1\n\t" ASTEND
          : "=&{v[64:95]}"(a), "+{v[96:111]}"(d), [t0] "=&v"(t0), [t1] "=&v"(t1), [todo] "+s"(todo), [cnt] "+s"(cnt), [msk] "=&s"(msk) ASTOUT
          : "{v[112:143]}"(f0), "{v[144:175]}"(f1), [po0] "v"(po[0]), [po1] "v"(po[1]), [po2] "v"(po[2]), [po3] "v"(po[3]), [pa] "v"(pa), [ca] "v"(ca),
            [idx] "v"(idx), [op] "v"(op), [pb] "s"(pb), [pob] "s"(pob), [yb] "s"(yb)
          : "memory", "vcc", "scc");
      ASTPUT(6, 0); ASTPUT(8, 1); ACNT(19, SPIN_MAX - cnt + 1);
    }
    STAMP(9);
    if (isG && YLATE) barrier();
    if (WOV) asm volatile("s_waitcnt vmcnt(1)" : "+{v[64:95]}"(a) : : "memory");
    if (isG) {
      barrier();
      if (gidx < 32 && lane < 8) st1_sc1(pa, ws + WS_H + (side ^ NPA) + gidx * 128, canon(*(LAS unsigned*)(sm + L_H + gidx * 32 + lane * 4)));
    } else if (isD) {
      unsigned hv;
      if (YLATE && !DPRE) barrier();
      _Pragma("nounroll") for (int it = 0; it < SPIN_MAX; ++it) {
        asm volatile("global_load_dword %0, %1, %2 sc1\n\ts_waitcnt vmcnt(0)" : "=&v"(hv) : "v"((unsigned)((c >> 1) * 4)), "s"(ws + WS_H + (side ^ NPA) + didx * 128) : "memory");
        if (!__any(hv == SENT)) break;
        __builtin_amdgcn_s_sleep(PB4);
      }
      if (YLATE && DPRE) barrier();
      STAMP(10);
      u32x4 dp;
      unsigned t0, t1;
      asm volatile("s_barrier\n\tds_read_b128 v[64:67], %[dr]\n\ts_waitcnt lgkmcnt(0)\n\t"
                   PKA("64:65", "64:65", "66:67") "s_nop 0\n\t" ROWSUM("%[t0]", "%[t1]", "v64", "v65")
                   "s_mov_b64 exec, 0xffff\n\tv_add_f32 %[t0], %[t0], %[h]\n\tv_cvt_pk_bf16_f32 %[t0], %[t0], %[t0]\n\t"
                   "global_store_short %[oa], %[t0], %[ob]\n\ts_mov_b64 exec, -1"
                   : "=&{v[64:67]}"(dp), [t0] "=&v"(t0), [t1] "=&v"(t1)
                   : [dr] "v"(sm + L_DP + c * 64 + r * 16), [h] "v"((c & 1) ? bfhi(hv) : bflo(hv)), [oa] "v"(c * 2), [ob] "s"(out + didx * 32) : "memory");
    }
    STAMP(12); STAMPR(13); STAMP_OUT();
  }
}

extern "C" void kernel_launch(void* const* d_in, const int* in_sizes, int n_in, void* d_out, int out_size, void* d_ws, size_t ws_size,
                              hipStream_t stream) {
  fused_decode_block<<<256, 384, 0, stream>>>((const char*)d_in[0], (const char*)d_in[1], (const char*)d_in[2], (const char*)d_in[3],
                                              (const char*)d_in[4], (const char*)d_in[6], (const char*)d_in[7], (const char*)d_in[8],
                                              (char*)d_out, (char*)d_ws);
}
```
